```python
import torch

def _softmax(x, axis=-1):
    return torch.softmax(x, dim=axis)

def _gelu(x):
    return torch.nn.functional.gelu(x, approximate='tanh')

def _silu(x):
    return torch.nn.functional.silu(x)

def _sigmoid(x):
    return torch.sigmoid(x)

def _layernorm(x, axis=-1, eps=1e-05):
    m = x.mean(dim=axis, keepdim=True)
    v = x.var(dim=axis, keepdim=True, unbiased=False)
    return (x - m) / torch.sqrt(v + eps)

def _scatter_add(tgt, idx, src, axis=0):
    return tgt.clone().index_add_(axis, idx, src)

def _segment_sum(src, seg, n):
    out = torch.zeros((n,) + src.shape[1:], dtype=src.dtype)
    return out.index_add_(0, seg, src)

def _pad(x, pw):
    flat = tuple((v for pair in reversed(pw) for v in pair))
    return torch.nn.functional.pad(x, flat)
kernel_name = 'parallel_sum__sliding_local_dilated__mixer_gmlp__prompt'

def setup_inputs():

    _w = lambda a: (lambda t: t.to(torch.bfloat16) if t.is_floating_point() else t)(torch.as_tensor(a))

    def _A_setup():
        B, S, D = (1, 2048, 576)
        return {'x': _w((lambda *s: torch.randn(*s, dtype=torch.bfloat16) * 0.05)(B, S, D)), 'wqkv': _w((lambda *s: torch.randn(*s, dtype=torch.bfloat16) * 0.2)(D, 3 * D)), 'wo': _w((lambda *s: torch.randn(*s, dtype=torch.bfloat16) * 0.1)(D, D))}
    _A_in = _A_setup()

    def _B_setup():
        B, S, D, F = (1, 2048, 576, 4608)
        Fh = F // 2
        return {'x': _w((lambda *s: torch.randn(*s, dtype=torch.bfloat16) * 0.05)(B, S, D)), 'w_in': _w((lambda *s: torch.randn(*s, dtype=torch.bfloat16) * 0.8)(D, 2 * Fh)), 'w_sp': _w(0.01 * (lambda *s: torch.randn(*s, dtype=torch.bfloat16) * 0.8)(S, S)), 'b_sp': _w(1.0 + 0.01 * (lambda *s: torch.randn(*s, dtype=torch.bfloat16) * 0.05)(S)), 'w_out': _w((lambda *s: torch.randn(*s, dtype=torch.bfloat16) * 0.05)(Fh, D))}
    _B_in = _B_setup()
    return {'A_x': _A_in['x'], 'A_wqkv': _A_in['wqkv'], 'A_wo': _A_in['wo'], 'B_x': _B_in['x'], 'B_w_in': _B_in['w_in'], 'B_w_sp': _B_in['w_sp'], 'B_b_sp': _B_in['b_sp'], 'B_w_out': _B_in['w_out']}

def reference(A_x, A_wqkv, A_wo, B_x, B_w_in, B_w_sp, B_b_sp, B_w_out):

    def _A_ref(x, wqkv, wo):
        B, S, D = x.shape
        H = 32
        Dh = D // H
        W, dil = (8, 2)
        qkv = (lambda x, *d: x.permute(*d))((x @ wqkv).reshape(B, S, 3, H, Dh), 2, 0, 3, 1, 4)
        q, k, v = (qkv[0], qkv[1], qkv[2])
        scale = Dh ** (-0.5)
        pos = torch.arange(S)
        off = torch.arange(W) * dil
        idx = pos[:, None] - off[None, :]
        mask = idx >= 0
        idx_c = torch.where(mask, idx, 0).reshape(-1)
        kg = k[:, :, idx_c, :].reshape(B, H, S, W, Dh)
        vg = v[:, :, idx_c, :].reshape(B, H, S, W, Dh)
        logits = torch.einsum('bhsd,bhswd->bhsw', q, kg) * scale
        logits = torch.where(mask[None, None, :, :], logits, -1000000000.0)
        attn = _softmax(logits, axis=-1)
        o = torch.einsum('bhsw,bhswd->bhsd', attn, vg)
        return (lambda x, *d: x.permute(*d))(o, 0, 2, 1, 3).reshape(B, S, D) @ wo

    def _B_ref(x, w_in, w_sp, b_sp, w_out):
        B, S, D = x.shape
        F = w_in.shape[-1]
        Fh = F // 2
        z = _gelu(torch.einsum('bsd,df->bsf', x, w_in))
        z1 = z[..., :Fh].clone()
        z2 = _layernorm(z[..., Fh:], axis=-1)
        g = torch.einsum('ts,bsf->btf', w_sp, z2) + b_sp[None, :, None]
        return torch.einsum('bsf,fd->bsd', z1 * g, w_out)
    x = A_x
    xn = _layernorm(x, axis=-1)
    return x + _A_ref(xn, A_wqkv, A_wo) + _B_ref(xn, B_w_in, B_w_sp, B_b_sp, B_w_out)
```

```cpp
#include <hip/hip_runtime.h>
#include <hip/hip_bf16.h>
#include <stdint.h>
typedef unsigned short u16;
__device__ __forceinline__ float b2f(u16 v){ return __uint_as_float(((unsigned)v)<<16); }
__device__ __forceinline__ u16 f2b(float f){ __hip_bfloat16 h = __float2bfloat16(f); return *(u16*)&h; }
__device__ __forceinline__ float rb(float f){ return b2f(f2b(f)); }

constexpr int S=2048, D=576, F=4608, FH=2304, H=32, DH=18;

__global__ void ln_kernel(const u16* x, u16* xn){
  int row=blockIdx.x; int t=threadIdx.x;
  float v[9]; float s=0;
  for(int i=0;i<9;i++){ v[i]=b2f(x[row*D+t+64*i]); s+=v[i]; }
  for(int o=32;o;o>>=1) s+=__shfl_xor(s,o);
  float m=s/D; float q=0;
  for(int i=0;i<9;i++){ float d=v[i]-m; q+=d*d; }
  for(int o=32;o;o>>=1) q+=__shfl_xor(q,o);
  float var=q/D;
  float mb=rb(m), vb=rb(var); float sd=rb(sqrtf(rb(vb+1e-5f)));
  for(int i=0;i<9;i++){ xn[row*D+t+64*i]=f2b(rb(v[i]-mb)/sd); }
}
template<int ACT>
__global__ void gemm_naive(const u16* A,const u16* B,u16* C,int M,int N,int K,int lda,int ldb,int ldc){
  __shared__ float As[16][65]; __shared__ float Bs[16][65];
  int tx=threadIdx.x&15, ty=threadIdx.x>>4; int bm=blockIdx.y*64,bn=blockIdx.x*64;
  float acc[4][4]={};
  for(int k0=0;k0<K;k0+=16){
    for(int i=threadIdx.x;i<64*16;i+=256){ int r=i>>4,c=i&15; As[c][r]=b2f(A[(size_t)(bm+r)*lda+k0+c]); }
    for(int i=threadIdx.x;i<64*16;i+=256){ int r=i>>6,c=i&63; Bs[r][c]=(bn+c<N)?b2f(B[(size_t)(k0+r)*ldb+bn+c]):0.f; }
    __syncthreads();
    for(int k=0;k<16;k++){ float a[4],b[4]; for(int i=0;i<4;i++){a[i]=As[k][ty*4+i]; b[i]=Bs[k][tx*4+i];}
      for(int i=0;i<4;i++)for(int j=0;j<4;j++)acc[i][j]+=a[i]*b[j]; }
    __syncthreads();
  }
  for(int i=0;i<4;i++)for(int j=0;j<4;j++){ int r=bm+ty*4+i,c=bn+tx*4+j; if(c<N){ float v=acc[i][j];
    if(ACT==1){ v=rb(v); float u=0.7978845608028654f*(v+0.044715f*v*v*v); v=0.5f*v*(1.f+tanhf(u)); }
    C[(size_t)r*ldc+c]=f2b(v);} }
}
__global__ void ln2_kernel(const u16* z, u16* z2n){
  int row=blockIdx.x; int t=threadIdx.x;
  __shared__ float red[8];
  float v[9]; float s=0;
  for(int i=0;i<9;i++){ v[i]=b2f(z[(size_t)row*F+FH+t+256*i]); s+=v[i]; }
  for(int o=32;o;o>>=1) s+=__shfl_xor(s,o);
  if((t&63)==0) red[t>>6]=s; __syncthreads();
  float m=(red[0]+red[1]+red[2]+red[3])/FH; float q=0;
  for(int i=0;i<9;i++){ float d=v[i]-m; q+=d*d; }
  for(int o=32;o;o>>=1) q+=__shfl_xor(q,o);
  if((t&63)==0) red[4+(t>>6)]=q; __syncthreads();
  float var=(red[4]+red[5]+red[6]+red[7])/FH;
  float mb=rb(m), vb=rb(var); float sd=rb(sqrtf(rb(vb+1e-5f)));
  for(int i=0;i<9;i++){ z2n[(size_t)row*FH+t+256*i]=f2b(rb(v[i]-mb)/sd); }
}
__global__ void gate_kernel(const u16* z,const u16* g,const u16* b,u16* h){
  int row=blockIdx.x;
  for(int c=threadIdx.x;c<FH;c+=blockDim.x){ float gg=rb(b2f(g[(size_t)row*FH+c])+b2f(b[row])); h[(size_t)row*(FH+D)+D+c]=f2b(b2f(z[(size_t)row*F+c])*gg); }
}
__global__ void attn_kernel(const u16* qkv,u16* o){
  int idx=blockIdx.x*blockDim.x+threadIdx.x; int s=idx/H,h=idx%H;
  float q[DH]; for(int d=0;d<DH;d++) q[d]=b2f(qkv[(size_t)s*3*D+h*DH+d]);
  float l[8]; float mx=-1e30f; const float scale=rb(0.23570226039551584f);
  for(int w=0;w<8;w++){ int p=s-2*w; if(p>=0){ float a=0; for(int d=0;d<DH;d++) a+=q[d]*b2f(qkv[(size_t)p*3*D+D+h*DH+d]); l[w]=rb(rb(a)*0.23570226039551584f);} else l[w]=-1e9f; mx=fmaxf(mx,l[w]); }
  float sum=0; for(int w=0;w<8;w++){ l[w]=__expf(l[w]-mx); sum+=l[w]; }
  float acc[DH]={};
  for(int w=0;w<8;w++){ int p=s-2*w; if(p<0)p=0; float a=rb(l[w]/sum); for(int d=0;d<DH;d++) acc[d]+=a*b2f(qkv[(size_t)p*3*D+2*D+h*DH+d]); }
  for(int d=0;d<DH;d++) o[(size_t)s*(FH+D)+h*DH+d]=f2b(acc[d]);
}
__global__ void final_kernel(const u16* x,const u16* a,const u16* b,u16* out,int n){
  int i=blockIdx.x*blockDim.x+threadIdx.x; if(i<n) out[i]=f2b(rb(b2f(x[i])+b2f(a[i]))+b2f(b[i]));
}
extern "C" void kernel_launch(void* const* d_in, const int* in_sizes, int n_in, void* d_out, int out_size, void* d_ws, size_t ws_size, hipStream_t stream) {
  const u16* x=(const u16*)d_in[0]; const u16* wqkv=(const u16*)d_in[1]; const u16* wo=(const u16*)d_in[2];
  const u16* w_in=(const u16*)d_in[4]; const u16* w_sp=(const u16*)d_in[5]; const u16* b_sp=(const u16*)d_in[6]; const u16* w_out=(const u16*)d_in[7];
  u16* ws=(u16*)d_ws;
  u16* xn=ws; u16* qkv=xn+S*D; u16* z=qkv+S*3*D; u16* z2n=z+(size_t)S*F; u16* g=z2n+(size_t)S*FH; u16* oh=g+(size_t)S*FH; u16* ya=oh+(size_t)S*(FH+D); u16* yb=ya+S*D;
  ln_kernel<<<S,64,0,stream>>>(x,xn);
  gemm_naive<0><<<dim3(27,32),256,0,stream>>>(xn,wqkv,qkv,S,3*D,D,D,3*D,3*D);
  gemm_naive<1><<<dim3(72,32),256,0,stream>>>(xn,w_in,z,S,F,D,D,F,F);
  ln2_kernel<<<S,256,0,stream>>>(z,z2n);
  gemm_naive<0><<<dim3(36,32),256,0,stream>>>(w_sp,z2n,g,S,FH,S,S,FH,FH);
  gate_kernel<<<S,256,0,stream>>>(z,g,b_sp,oh);
  attn_kernel<<<S*H/256,256,0,stream>>>(qkv,oh);
  gemm_naive<0><<<dim3(9,32),256,0,stream>>>(oh,wo,ya,S,D,D,FH+D,D,D);
  gemm_naive<0><<<dim3(9,32),256,0,stream>>>(oh+D,w_out,yb,S,D,FH,FH+D,D,D);
  final_kernel<<<S*D/256,256,0,stream>>>(x,ya,yb,(u16*)d_out,S*D);
}
```

```cpp
#include <hip/hip_runtime.h>
#include <hip/hip_bf16.h>
#include <stdint.h>
typedef unsigned short u16;
typedef __attribute__((ext_vector_type(8))) __bf16 bf16x8;
typedef __attribute__((ext_vector_type(4))) float f32x4;
typedef __attribute__((ext_vector_type(4))) unsigned u32x4;
typedef __attribute__((ext_vector_type(2))) unsigned u32x2;
constexpr int S=2048, D=576, F=4608, FH=2304, H=32, DH=18;
__device__ __forceinline__ float b2f(u16 v){ return __uint_as_float(((unsigned)v)<<16); }
__device__ __forceinline__ float lo2f(unsigned v){ return __uint_as_float(v<<16); }
__device__ __forceinline__ float hi2f(unsigned v){ return __uint_as_float(v&0xffff0000u); }
__device__ __forceinline__ unsigned pack2(float a,float b){ typedef __attribute__((ext_vector_type(2))) __bf16 bf2; typedef __attribute__((ext_vector_type(2))) float f2; f2 f={a,b}; bf2 r=__builtin_convertvector(f,bf2); return __builtin_bit_cast(unsigned,r); }
__device__ __forceinline__ u16 f2b(float f){ return (u16)(pack2(f,0.f)&0xffffu); }
__device__ __forceinline__ float rb(float f){ return lo2f(pack2(f,0.f)); }
__device__ __forceinline__ __amdgpu_buffer_rsrc_t mk_srd(const void* p,unsigned bytes){ return __builtin_amdgcn_make_buffer_rsrc((void*)p,(short)0,(int)bytes,0x00020000); }
__device__ __forceinline__ void dma16(__amdgpu_buffer_rsrc_t srd,unsigned voff,unsigned soff,unsigned ldsaddr){
  asm volatile("s_mov_b32 m0, %0\n\ts_nop 0\n\tbuffer_load_dwordx4 %1, %2, %3 offen lds" :: "s"(ldsaddr),"v"(voff),"s"(srd),"s"(soff) : "memory");
}
__device__ __forceinline__ void dma16_sc1(__amdgpu_buffer_rsrc_t srd,unsigned voff,unsigned soff,unsigned ldsaddr){
  asm volatile("s_mov_b32 m0, %0\n\ts_nop 0\n\tbuffer_load_dwordx4 %1, %2, %3 offen sc1 lds" :: "s"(ldsaddr),"v"(voff),"s"(srd),"s"(soff) : "memory");
}
#define WAITV(n) asm volatile("s_waitcnt vmcnt(" #n ")" ::: "memory")
#define BARRIER() asm volatile("s_barrier" ::: "memory")
#define WAITL0_BARRIER() asm volatile("s_waitcnt lgkmcnt(0)\n\ts_barrier" ::: "memory")
template<int V> struct IC{ static constexpr int value=V; };
template<int MODE> __device__ __forceinline__ void ST16(void* p,u32x4 v){
  if constexpr(MODE==0) *(u32x4*)p=v;
  else asm volatile("global_store_dwordx4 %0, %1, off sc1\n\ts_nop 2" :: "v"(p),"v"(v) : "memory");
}
#define SM0 0
#define SM1A 0
#define SM1B 2
#define SM2 2
#define MFMA(acc,a,b) asm volatile("v_mfma_f32_16x16x32_bf16 %0, %1, %2, %0" : "+v"(acc) : "v"(a), "v"(b))
#define LDSRD(dst,addr,off) asm volatile("ds_read_b128 %0, %1 offset:%2" : "=v"(dst) : "v"(addr), "n"(off))
#define WAITL0() asm volatile("s_waitcnt lgkmcnt(0)" ::: "memory")
#define MFMA_DRAIN() do{ __builtin_amdgcn_sched_barrier(0); asm volatile("s_nop 15\n\ts_nop 7" ::: "memory"); __builtin_amdgcn_sched_barrier(0); }while(0)
template<int NN,bool TR,bool RD>
__device__ __forceinline__ void mphase(f32x4 (&acc)[9][2],const bf16x8 (&ca)[2],const bf16x8 (&cb)[9],bf16x8 (&na)[2],bf16x8 (&nb)[9],unsigned pa,unsigned pb){
  #pragma unroll
  for(int n=0;n<NN;n++){
    if(TR){ MFMA(acc[n][0],ca[0],cb[n]); MFMA(acc[n][1],ca[1],cb[n]); }
    else  { MFMA(acc[n][0],cb[n],ca[0]); MFMA(acc[n][1],cb[n],ca[1]); }
    if(RD){
      if(n==0){ LDSRD(na[0],pa,0); LDSRD(na[1],pa,2048); }
      else { if(2*n-2<NN) LDSRD(nb[2*n-2<NN?2*n-2:0],pb,(2*n-2)*2048); if(2*n-1<NN) LDSRD(nb[2*n-1<NN?2*n-1:0],pb,(2*n-1)*2048); }
    }
  }
}
__device__ __forceinline__ float gelu_f(float x){
  const float c0=-2.3022081986f, c1=-0.1029432397f;
  float e=__builtin_amdgcn_exp2f(x*__builtin_fmaf(x*x,c1,c0));
  return x*__builtin_amdgcn_rcpf(1.f+e);
}
#define DOT2(acc,a,b) asm("v_dot2c_f32_bf16 %0, %1, %2" : "+v"(acc) : "v"(a), "v"(b))
#define PIN(x) asm volatile("" : "+v"(x))
template<int I,int N,class Fn> __device__ __forceinline__ void sfor(Fn&& f){ if constexpr(I<N){ f(IC<I>{}); sfor<I+1,N>(f); } }
__device__ __forceinline__ float amul(float a,float b){ float r; asm("v_mul_f32 %0, %1, %2" : "=v"(r) : "v"(a),"v"(b)); return r; }
__device__ __forceinline__ float aadd(float a,float b){ float r; asm("v_add_f32 %0, %1, %2" : "=v"(r) : "v"(a),"v"(b)); return r; }
__device__ __forceinline__ float afma(float a,float b,float c){ float r; asm("v_fma_f32 %0, %1, %2, %3" : "=v"(r) : "v"(a),"v"(b),"v"(c)); return r; }
__device__ __forceinline__ float gelu_s(float x){
  const float c0=-2.3022081986f, c1=-0.1029432397f;
  float e=__builtin_amdgcn_exp2f(amul(x,__builtin_fmaf(amul(x,x),c1,c0)));
  return amul(x,__builtin_amdgcn_rcpf(1.f+e));
}
typedef __attribute__((ext_vector_type(2))) float f32x2;
__device__ __forceinline__ f32x2 gelu2(f32x2 x){
  const f32x2 c0={-2.3022081986f,-2.3022081986f}, c1={-0.1029432397f,-0.1029432397f}, one={1.f,1.f};
  f32x2 t=x*__builtin_elementwise_fma(x*x,c1,c0);
  f32x2 e; e[0]=__builtin_amdgcn_exp2f(t[0]); e[1]=__builtin_amdgcn_exp2f(t[1]); e=e+one;
  f32x2 r; r[0]=__builtin_amdgcn_rcpf(e[0]); r[1]=__builtin_amdgcn_rcpf(e[1]);
  return x*r;
}
__device__ __forceinline__ float rowsum16(float v){
  v+=__builtin_bit_cast(float,__builtin_amdgcn_update_dpp(0,__builtin_bit_cast(int,v),0x128,0xf,0xf,false));
  v+=__builtin_bit_cast(float,__builtin_amdgcn_update_dpp(0,__builtin_bit_cast(int,v),0x124,0xf,0xf,false));
  v+=__builtin_bit_cast(float,__builtin_amdgcn_update_dpp(0,__builtin_bit_cast(int,v),0x122,0xf,0xf,false));
  v+=__builtin_bit_cast(float,__builtin_amdgcn_update_dpp(0,__builtin_bit_cast(int,v),0x121,0xf,0xf,false));
  return v;
}
constexpr int LD3 = 2944;
constexpr int CGROWS = 400;
namespace k0 {
constexpr int NT_WIN=648, NT_QKV=243, NT=NT_WIN+NT_QKV+1;
#define K0TPB 2
constexpr int TPB=K0TPB;
constexpr int NB_LN=512, NB_SENT=64, NB_PADA=64, NB_PADW3=18, NB_PADW1=18;
constexpr int GRID=NT/TPB+NB_LN+NB_SENT+NB_PADA+NB_PADW3+NB_PADW1;
__global__ __launch_bounds__(256) void kernel(const u16* __restrict__ x,const u16* __restrict__ wqkv,const u16* __restrict__ win,
   u16* __restrict__ xn,u16* __restrict__ w1t,u16* __restrict__ w3t,u16* __restrict__ a3,u32x4* __restrict__ sent){
  __shared__ unsigned tile[TPB*64*33];
  int b=blockIdx.x; const int tid=threadIdx.x;

  if(b<NT/TPB){
    u32x4 v[TPB][2]; int kind[TPB],k0[TPB],n0[TPB];
    #pragma unroll
    for(int u=0;u<TPB;u++){ int tb=b*TPB+u; const u16* src; int ldn;
      if(tb<NT_WIN){ kind[u]=0; src=win; ldn=F; k0[u]=(tb%9)*64; n0[u]=(tb/9)*64; }
      else { tb=min(tb-NT_WIN,NT_QKV-1); kind[u]=1; src=wqkv; ldn=3*D; k0[u]=(tb%9)*64; n0[u]=(tb/9)*64; }
      #pragma unroll
      for(int i=0;i<2;i++){ const int r=(tid>>3)+32*i, c=tid&7; v[u][i]=*(const u32x4*)(src+(size_t)(k0[u]+r)*ldn+n0[u]+c*8); } }
    #pragma unroll
    for(int u=0;u<TPB;u++)
      #pragma unroll
      for(int i=0;i<2;i++){ const int r=(tid>>3)+32*i, c=tid&7; unsigned* t=tile+u*(64*33)+r*33+c*4;
        t[0]=v[u][i][0]; t[1]=v[u][i][1]; t[2]=v[u][i][2]; t[3]=v[u][i][3]; }
    __syncthreads();
    #pragma unroll
    for(int u=0;u<TPB;u++){ const u16* t16=(const u16*)(tile+u*(64*33));
      #pragma unroll
      for(int i=0;i<2;i++){ const int n=(tid>>3)+32*i, kc=tid&7;
        unsigned e[8];
        #pragma unroll
        for(int j=0;j<8;j++) e[j]=t16[(kc*8+j)*66+n];
        u32x4 w; w[0]=e[0]|(e[1]<<16); w[1]=e[2]|(e[3]<<16); w[2]=e[4]|(e[5]<<16); w[3]=e[6]|(e[7]<<16);
        const int ng=n0[u]+n; u16* dst;
        if(kind[u]==0){ int f=ng; int off=144; if(f>=FH){ f-=FH; off=0; } const int c=f/144; dst=w1t+(size_t)(c*CGROWS+off+(f-c*144))*D+k0[u]; }
        else { const int part=ng/D, rem=ng-part*D, hd=rem/DH, d=rem-hd*DH; dst=w1t+(size_t)((hd>>1)*CGROWS+288+part*36+(hd&1)*18+d)*D+k0[u]; }
        ST16<SM0>(dst+kc*8,w); } }

    return;
  }
  b-=NT/TPB;
  if(b<NB_LN){
    const int row=b*4+(tid>>6), lane=tid&63;
    const unsigned* xr=(const unsigned*)(x+(size_t)row*D);
    unsigned v[5]; float s=0;
    #pragma unroll
    for(int i=0;i<5;i++){ const int c=lane+64*i; v[i]=(c<288)?xr[c]:0u; s+=lo2f(v[i])+hi2f(v[i]); }
    #pragma unroll
    for(int o=32;o;o>>=1) s+=__shfl_xor(s,o);
    const float m=s*(1.f/D); float q=0;
    #pragma unroll
    for(int i=0;i<5;i++){ const int c=lane+64*i; if(c<288){ float a=lo2f(v[i])-m, bb=hi2f(v[i])-m; q+=a*a+bb*bb; } }
    #pragma unroll
    for(int o=32;o;o>>=1) q+=__shfl_xor(q,o);
    const float mb=rb(m), sd=rb(sqrtf(rb(rb(q*(1.f/D))+1e-5f)));
    unsigned* xo=(unsigned*)(xn+(size_t)row*D);
    #pragma unroll
    for(int i=0;i<5;i++){ const int c=lane+64*i; if(c<288) xo[c]=pack2(rb(lo2f(v[i])-mb)/sd,rb(hi2f(v[i])-mb)/sd); }

    return;
  }
  b-=NB_LN;

  const u32x4 z4={0,0,0,0};
  if(b<NB_SENT){ const u32x4 f4={~0u,~0u,~0u,~0u}; sent[b*256+tid]=f4; return; }
  b-=NB_SENT;
  if(b<NB_PADA){ const int i=b*256+tid; *(u32x4*)(a3+(size_t)(i>>3)*LD3+2880+(i&7)*8)=z4; return; }
  b-=NB_PADA;
  if(b<NB_PADW3){ const int i=b*256+tid; *(u32x4*)(w3t+(size_t)(i>>3)*LD3+2880+(i&7)*8)=z4; return; }
  b-=NB_PADW3;
  { const int i=b*256+tid; const int r=i/72, c=i-r*72;
    *(u32x4*)(w1t+(size_t)((r>>2)*CGROWS+396+(r&3))*D+c*8)=z4; }
}
}
namespace k1 {
constexpr int SLOT=34816, NSLOT=3, SOFF=NSLOT*SLOT, SBYTES=144*304, ROFF=SOFF+SBYTES, STOFF=ROFF+4096, LDS_BYTES=STOFF+1024;
#define SB() __builtin_amdgcn_sched_barrier(0)
__global__ __launch_bounds__(512,2) void kernel(const u16* __restrict__ xn,const u16* __restrict__ w1t,u16* __restrict__ z2t,
                                              u16* __restrict__ a3,unsigned long long* __restrict__ part,const u16* __restrict__ wsp){
  extern __shared__ __attribute__((aligned(16))) char shm[];
  const int tid=threadIdx.x, wid=__builtin_amdgcn_readfirstlane(tid>>6), lane=tid&63;
  const int b=blockIdx.x, xcd=b&7, i0=b>>3;
  const int tb=(xcd&1)*8+(i0&7), cg=(xcd>>1)*4+(i0>>3);
  const unsigned ldsbase=(unsigned)(size_t)shm;
  char* Sb=shm+SOFF; float2* red=(float2*)(shm+ROFF); float* sttf=(float*)(shm+STOFF);

  if(wid>=4){
    const int lw=wid-4, ltid=tid-256;
    __amdgpu_buffer_rsrc_t srdA=mk_srd(xn+((ptrdiff_t)tb*128-16)*D, 144*D*2);
    __amdgpu_buffer_rsrc_t srdB=mk_srd(w1t+(size_t)cg*CGROWS*D, CGROWS*D*2);
    const int sw=((lw&1)<<2)|(lane>>4);
    const unsigned voff=(unsigned)((lane>>3)*(D*2)+(((lane&7)^sw)<<4));
    auto piece=[&](int g,int j) __attribute__((always_inline)) {
      if(g>=18) return;
      const int sub=g/9, kt=g-sub*9;
      const unsigned slot=ldsbase+(unsigned)(g%NSLOT)*SLOT;
      if(sub!=1){
        if(j<4){ const int p=lw+4*j; dma16(srdA,voff,(unsigned)(16*D*2+kt*128)+p*(8u*D*2),slot+p*1024u); }
        else { const int p=lw+4*(j-4); if(j<8||p<18) dma16(srdB,voff,(unsigned)(kt*128)+p*(8u*D*2),slot+16384u+p*1024u); }
      } else {
        if(j<4){ const int p=lw+4*j; dma16(srdA,voff,(unsigned)(kt*128)+p*(8u*D*2),slot+p*1024u); }
        else if(j<7){ const int p=((lw+2)&3)+4*(j-4); dma16(srdB,voff,(unsigned)(288*D*2+kt*128)+p*(8u*D*2),slot+18432u+p*1024u); }
        else if(j==7){ if(lw<2){ const int p=lw+16; dma16(srdA,voff,(unsigned)(kt*128)+p*(8u*D*2),slot+p*1024u); }
                       else { const int p=((lw+2)&3)+12; dma16(srdB,voff,(unsigned)(288*D*2+kt*128)+p*(8u*D*2),slot+18432u+p*1024u); } }
      }
    };
    auto issue=[&](int g) __attribute__((always_inline)) {
      #pragma unroll
      for(int j=0;j<9;j++) piece(g,j); };
    auto waitstage=[&](int g) __attribute__((always_inline)) {
      if(g+2<18){ if(g+2>=9) WAITV(8); else { if(lw<2) WAITV(9); else WAITV(8); } } else WAITV(0); };
    issue(0); issue(1); issue(2); if(lw<2) WAITV(18); else WAITV(16);
    BARRIER();
    #pragma unroll
    for(int g=0;g<9;g++){ waitstage(g); BARRIER(); issue(g+3); }
    const int h=lw&1, sl=(lw>>1)*64+lane, sg=tb*128+sl;
    unsigned q[9]; float l[8]; unsigned pp[4]; float o[18];
    auto rowof=[&](int w) __attribute__((always_inline)) { return (sg-2*w>=0)?(16+sl-2*w):16; };
    auto logits4=[&](int w0,int g) __attribute__((always_inline)) {
      u32x4 a[4],c[4]; unsigned e[4];
      #pragma unroll
      for(int i=0;i<4;i++){ const char* p=Sb+rowof(w0+i)*304+(2+h)*48; a[i]=*(const u32x4*)p; c[i]=*(const u32x4*)(p+16); e[i]=*(const unsigned*)(p+32); }
      #pragma unroll
      for(int j=0;j<5;j++) piece(g,j);
      float d[4],d2[4];
      #pragma unroll
      for(int i=0;i<4;i++){ d[i]=0.f; d2[i]=0.f; }
      #pragma unroll
      for(int t=0;t<4;t++)
        #pragma unroll
        for(int i=0;i<4;i++){ DOT2(d[i],q[t],a[i][t]); DOT2(d2[i],q[4+t],c[i][t]); }
      #pragma unroll
      for(int i=0;i<4;i++) DOT2(d[i],q[8],e[i]);
      asm volatile("s_nop 2" : "+v"(d[0]),"+v"(d[1]),"+v"(d[2]),"+v"(d[3]),"+v"(d2[0]),"+v"(d2[1]),"+v"(d2[2]),"+v"(d2[3]));
      #pragma unroll
      for(int i=0;i<4;i++){ l[w0+i]=(sg-2*(w0+i)>=0)?rb(rb(d[i]+d2[i])*0.23570226039551584f):-1e9f; PIN(l[w0+i]); }
      #pragma unroll
      for(int j=5;j<9;j++) piece(g,j); };
    auto pv2=[&](int i0_,int g) __attribute__((always_inline)) {
      u32x4 a[4],c[4]; unsigned e[4];
      #pragma unroll
      for(int i=0;i<4;i++){ const char* p=Sb+rowof(2*i0_+i)*304+(4+h)*48; a[i]=*(const u32x4*)p; c[i]=*(const u32x4*)(p+16); e[i]=*(const unsigned*)(p+32); }
      #pragma unroll
      for(int j=0;j<5;j++) piece(g,j);
      #pragma unroll
      for(int i=0;i<2;i++){
        #pragma unroll
        for(int t=0;t<4;t++){
          { const unsigned lo=__builtin_amdgcn_perm(a[2*i+1][t],a[2*i][t],0x05040100u), hi=__builtin_amdgcn_perm(a[2*i+1][t],a[2*i][t],0x07060302u); DOT2(o[2*t],lo,pp[i0_+i]); DOT2(o[2*t+1],hi,pp[i0_+i]); }
          { const unsigned lo=__builtin_amdgcn_perm(c[2*i+1][t],c[2*i][t],0x05040100u), hi=__builtin_amdgcn_perm(c[2*i+1][t],c[2*i][t],0x07060302u); DOT2(o[8+2*t],lo,pp[i0_+i]); DOT2(o[9+2*t],hi,pp[i0_+i]); } }
        { const unsigned lo=__builtin_amdgcn_perm(e[2*i+1],e[2*i],0x05040100u), hi=__builtin_amdgcn_perm(e[2*i+1],e[2*i],0x07060302u); DOT2(o[16],lo,pp[i0_+i]); DOT2(o[17],hi,pp[i0_+i]); } }
      asm volatile("s_nop 2" : "+v"(o[0]),"+v"(o[1]),"+v"(o[2]),"+v"(o[3]),"+v"(o[4]),"+v"(o[5]),"+v"(o[6]),"+v"(o[7]),"+v"(o[8]));
      asm volatile("s_nop 2" : "+v"(o[9]),"+v"(o[10]),"+v"(o[11]),"+v"(o[12]),"+v"(o[13]),"+v"(o[14]),"+v"(o[15]),"+v"(o[16]),"+v"(o[17]));
      #pragma unroll
      for(int j=5;j<9;j++) piece(g,j); };
    #pragma unroll
    for(int kt=0;kt<9;kt++){ const int g=9+kt;
      if(kt<8) waitstage(g);
      BARRIER();
      issue(g+3);
    }
    BARRIER();

    { const char* p=Sb+(16+sl)*304+h*48; const u32x4 a=*(const u32x4*)p, c=*(const u32x4*)(p+16); q[0]=a[0];q[1]=a[1];q[2]=a[2];q[3]=a[3];q[4]=c[0];q[5]=c[1];q[6]=c[2];q[7]=c[3]; q[8]=*(const unsigned*)(p+32); }
    logits4(0,18); logits4(4,18);
    { float mx=l[0];
      #pragma unroll
      for(int w=1;w<8;w++) mx=fmaxf(mx,l[w]);
      float sum=0.f;
      #pragma unroll
      for(int w=0;w<8;w++){ l[w]=__builtin_amdgcn_exp2f((l[w]-mx)*1.4426950408889634f); sum+=l[w]; }
      const float inv=1.f/sum;
      #pragma unroll
      for(int i=0;i<4;i++) pp[i]=pack2(l[2*i]*inv,l[2*i+1]*inv);
      #pragma unroll
      for(int t=0;t<18;t++) o[t]=0.f; }
    pv2(0,18); pv2(2,18);

    { typedef unsigned u32x4a __attribute__((ext_vector_type(4),aligned(4)));
      unsigned* op=(unsigned*)(a3+(size_t)sg*LD3+(cg*2+h)*DH);
      *(u32x4a*)op=u32x4a{pack2(o[0],o[1]),pack2(o[2],o[3]),pack2(o[4],o[5]),pack2(o[6],o[7])};
      *(u32x4a*)(op+4)=u32x4a{pack2(o[8],o[9]),pack2(o[10],o[11]),pack2(o[12],o[13]),pack2(o[14],o[15])};
      op[8]=pack2(o[16],o[17]); }

  } else {
    const int fr=lane&15, fq=lane>>4;
    const unsigned o0=(unsigned)(fr*128+(((0+fq)^(fr>>1))<<4)), o1=(unsigned)(fr*128+(((4+fq)^(fr>>1))<<4));
    f32x4 acc[9][2]; bf16x8 xa[2],xb[9],ya[2],yb[9];
    BARRIER();

    auto gemm9=[&](auto subc) __attribute__((always_inline)) {
      constexpr int sub=decltype(subc)::value; constexpr bool TR=true;
      #pragma unroll
      for(int n=0;n<9;n++){ acc[n][0]=f32x4{0,0,0,0}; acc[n][1]=f32x4{0,0,0,0}; }
      const unsigned a0=ldsbase+wid*4096+o0, a1=ldsbase+wid*4096+o1, b0=ldsbase+16384+o0, b1=ldsbase+16384+o1;
      { const unsigned so=((sub*9)%NSLOT)*SLOT; const unsigned pa=a0+so, pb=b0+so;
        LDSRD(xa[0],pa,0); LDSRD(xa[1],pa,2048);
        #pragma unroll
        for(int n=0;n<9;n++) LDSRD(xb[n],pb,n*2048);
        WAITL0(); }
      #pragma unroll
      for(int kt=0;kt<9;kt++){
        const int g=sub*9+kt; const unsigned so=(g%NSLOT)*SLOT, sn=((g+1)%NSLOT)*SLOT;
        mphase<9,TR,true>(acc,xa,xb,ya,yb,a1+so,b1+so);
        WAITL0_BARRIER();
        if(kt<8){ mphase<9,TR,true>(acc,ya,yb,xa,xb,a0+sn,b0+sn); WAITL0(); }
        else mphase<9,TR,false>(acc,ya,yb,xa,xb,0,0);
      }
      MFMA_DRAIN(); };
    unsigned pfd=0; const char* pfa=(const char*)wsp+(size_t)b*32768+tid*128;
    gemm9(IC<0>{});

    asm volatile("global_load_dword %0, %1, off" : "+v"(pfd) : "v"(pfa) : "memory");
    unsigned stash[9][2][2];
    { f32x2 sm[2][2],sq[2][2];
      #pragma unroll
      for(int m=0;m<2;m++){ sm[m][0]=f32x2{0,0}; sm[m][1]=f32x2{0,0}; sq[m][0]=f32x2{0,0}; sq[m][1]=f32x2{0,0}; }
      #pragma unroll
      for(int n=0;n<9;n++)
        #pragma unroll
        for(int m=0;m<2;m++)
          #pragma unroll
          for(int e=0;e<2;e++){ const f32x2 y=gelu2(f32x2{acc[n][m][2*e],acc[n][m][2*e+1]}); stash[n][m][e]=pack2(y[0],y[1]);
            sm[m][e]+=y; sq[m][e]=__builtin_elementwise_fma(y,y,sq[m][e]); }
      #pragma unroll
      for(int m=0;m<2;m++)
        #pragma unroll
        for(int j=0;j<4;j++){ const float s_=rowsum16(sm[m][j>>1][j&1]), q_=rowsum16(sq[m][j>>1][j&1]);
          const unsigned long long u=((unsigned long long)min(__float_as_uint(q_),0xfffffffeu)<<32)|min(__float_as_uint(s_),0xfffffffeu);
          if(fr==0) __hip_atomic_store(part+(size_t)(tb*16+cg)*128+wid*32+m*16+fq*4+j,u,__ATOMIC_RELAXED,__HIP_MEMORY_SCOPE_AGENT); }
      #pragma unroll
      for(int n=0;n<9;n++){ PIN(stash[n][0][0]); PIN(stash[n][0][1]); PIN(stash[n][1][0]); PIN(stash[n][1][1]); }
    }

    const int sr=lane&31, hf=lane>>5;
    const unsigned long long* pp_=part+(size_t)(tb*16+hf*8)*128+wid*32+sr;
    unsigned long long pv[8];
    { f32x4 hacc[2];
      #pragma unroll
      for(int n=0;n<7;n++){ acc[n][0]=f32x4{0,0,0,0}; acc[n][1]=f32x4{0,0,0,0}; }
      hacc[0]=f32x4{0,0,0,0}; hacc[1]=f32x4{0,0,0,0};
      bf16x8 xh,xg,yh,yg;
      const unsigned a0=ldsbase+2048+wid*4096+o0, a1=ldsbase+2048+wid*4096+o1, b0=ldsbase+18432+o0, b1=ldsbase+18432+o1;
      const unsigned h0=ldsbase+o0, h1=ldsbase+o1, g0=b0+(2+wid)*2048, g1=b1+(2+wid)*2048;
      { const unsigned so=(9%NSLOT)*SLOT; const unsigned pa=a0+so, pb=b0+so;
        LDSRD(xa[0],pa,0); LDSRD(xa[1],pa,2048);
        #pragma unroll
        for(int n=0;n<7;n++) LDSRD(xb[n],pb,n*2048);
        LDSRD(xh,h0+so,0); LDSRD(xg,g0+so,0);
        WAITL0(); }
      #pragma unroll
      for(int kt=0;kt<9;kt++){
        const int g=9+kt; const unsigned so=(g%NSLOT)*SLOT, sn=((g+1)%NSLOT)*SLOT;
        { const unsigned ph=h1+so, pg=g1+so;
          mphase<7,false,true>(acc,xa,xb,ya,yb,a1+so,b1+so);
          MFMA(hacc[0],xg,xh); LDSRD(yh,ph,0); LDSRD(yg,pg,0); MFMA(hacc[1],xb[6],xh); }
        WAITL0_BARRIER();
        if(kt<8){ const unsigned ph=h0+sn, pg=g0+sn;
          mphase<7,false,true>(acc,ya,yb,xa,xb,a0+sn,b0+sn);
          MFMA(hacc[0],yg,yh); LDSRD(xh,ph,0); LDSRD(xg,pg,0); MFMA(hacc[1],yb[6],yh); WAITL0(); }
        else { mphase<7,false,false>(acc,ya,yb,xa,xb,0,0); MFMA(hacc[0],yg,yh); MFMA(hacc[1],yb[6],yh); }
      }
      MFMA_DRAIN();

      asm volatile("s_waitcnt vmcnt(0)" :: "v"(pfd) : "memory");
      #pragma unroll
      for(int i=0;i<8;i++) pv[i]=__hip_atomic_load(pp_+i*128,__ATOMIC_RELAXED,__HIP_MEMORY_SCOPE_AGENT);
      #pragma unroll
      for(int n=0;n<7;n++)
        #pragma unroll
        for(int e=0;e<2;e++){ const int col=n*16+fq*4+2*e; const int grp=(col*3641)>>16; const int po=grp*48+(col-grp*18)*2;
          if(n<6||col<108){
            #pragma unroll
            for(int m=0;m<2;m++) *(unsigned*)(Sb+(16+wid*32+m*16+fr)*304+po)=pack2(acc[n][m][2*e],acc[n][m][2*e+1]); } }
      #pragma unroll
      for(int e=0;e<2;e++){ { const int col=(2+wid)*16+fq*4+2*e; const int grp=(col*3641)>>16; const int po=grp*48+(col-grp*18)*2;
          *(unsigned*)(Sb+fr*304+po)=pack2(hacc[0][2*e],hacc[0][2*e+1]); }
        if(wid==0){ const int col=96+fq*4+2*e; if(col<108){ const int grp=(col*3641)>>16; const int po=grp*48+(col-grp*18)*2;
          *(unsigned*)(Sb+fr*304+po)=pack2(hacc[1][2*e],hacc[1][2*e+1]); } } }
      WAITL0_BARRIER();

    }
    char* Z2=shm+wid*9216;
    auto do_stats=[&]() __attribute__((always_inline)) {
      { bool miss=false;
        #pragma unroll
        for(int i=0;i<8;i++) miss|=((unsigned)pv[i]==~0u)|((unsigned)(pv[i]>>32)==~0u);
        if(__builtin_amdgcn_ballot_w64(miss)!=0){
          #pragma unroll
          for(int i=0;i<8;i++) while((unsigned)pv[i]==~0u||(unsigned)(pv[i]>>32)==~0u) pv[i]=__hip_atomic_load(pp_+i*128,__ATOMIC_RELAXED,__HIP_MEMORY_SCOPE_AGENT); }
        float s_=0.f,q_=0.f;
        #pragma unroll
        for(int i=0;i<8;i++){ s_+=__uint_as_float((unsigned)pv[i]); q_+=__uint_as_float((unsigned)(pv[i]>>32)); }
        s_+=__shfl_xor(s_,32); q_+=__shfl_xor(q_,32);
        const float mean=s_*(1.f/FH); const float var=fmaxf(q_*(1.f/FH)-mean*mean,0.f); const float rs=__builtin_amdgcn_rsqf(var+1e-5f);
        if(hf==0){ sttf[wid*32+sr]=rs; sttf[128+wid*32+sr]=-mean*rs; }
        asm volatile("s_waitcnt lgkmcnt(0)" ::: "memory"); }
    };
    auto do_z2=[&]() __attribute__((always_inline)) {
      #pragma unroll
      for(int m=0;m<2;m++){
        const f32x4 rs=*(const f32x4*)(sttf+wid*32+m*16+fq*4), nm=*(const f32x4*)(sttf+128+wid*32+m*16+fq*4);
        char* zw=Z2+fr*64+(((m*2+(fq>>1))^(fr>>2))<<4)+(fq&1)*8;
        #pragma unroll
        for(int n=0;n<9;n++){ u32x2 v;
          #pragma unroll
          for(int e=0;e<2;e++){ const f32x2 z={lo2f(stash[n][m][e]),hi2f(stash[n][m][e])};
            const f32x2 y=__builtin_elementwise_fma(z,f32x2{rs[2*e],rs[2*e+1]},f32x2{nm[2*e],nm[2*e+1]}); v[e]=pack2(y[0],y[1]); }
          *(u32x2*)(zw+n*1024)=v; } }
      asm volatile("s_waitcnt lgkmcnt(0)" ::: "memory");

      { const int c=lane&3, f0=lane>>2; const char* zr=Z2+f0*64+((c^(f0>>2))<<4);
        u16* zg=z2t+(size_t)(cg*144+f0)*S+tb*128+wid*32+c*8;
        #pragma unroll
        for(int i=0;i<9;i++) ST16<SM1B>(zg+(size_t)i*16*S,*(const u32x4*)(zr+i*1024)); } };
    do_stats(); do_z2();
  }

}
}
namespace k2 {
constexpr int SLOT = 34*1024, NSLOT = 4, TOFF = NSLOT*SLOT, LDS_BYTES = TOFF+4*4352, NZ = 9, NT = 32+NZ;
__device__ __forceinline__ int korder(int t, int r){
  int d=t-r; bool lead=(d>=0)&&((d&3)==0)&&(t<=28);
  int nl=(d>=0)?min((d>>2)+1,((28-r)>>2)+1):0;
  int m=t-nl; int mp=m-(r+3);
  int k=(mp<0)?m:(r+4+mp+(mp*43>>7));
  return lead?(t+3):k;
}
__global__ __launch_bounds__(512,2) void kernel(const u16* __restrict__ wsp,const u16* __restrict__ z2t,const u16* __restrict__ xn,const u16* __restrict__ w1t,
                                              const u16* __restrict__ bsp,u16* __restrict__ hout,
                                              const u16* __restrict__ wo,const u16* __restrict__ wout,u16* __restrict__ w3t){
  constexpr int ldh=LD3;
  extern __shared__ __attribute__((aligned(16))) char shm[];
  const int wid=__builtin_amdgcn_readfirstlane(threadIdx.x>>6), lane=threadIdx.x&63;
  const int b=blockIdx.x, xcd=b&7, i=b>>3;
  const int ia=i&3, ib=i>>2;
  const int tb=(xcd&3)*4+ia, fb=(xcd>>2)*8+ib;
  const unsigned ldsbase=(unsigned)(size_t)shm;

  auto store_tile=[&]() __attribute__((always_inline)) {
    u16* hb=hout+(size_t)tb*128*ldh+fb*144;
    #pragma unroll
    for(int j=0;j<5;j++){ const int c=threadIdx.x+512*j; if(c<2304){ const int r=(c*3641)>>16; const int cc=c-r*18;
      u32x4 v=*(const u32x4*)(shm+c*16); ST16<SM2>(hb+(size_t)r*ldh+cc*8,v); } } };
  if(wid>=4){
    const int lw=wid-4, ltid=threadIdx.x-256;
    __amdgpu_buffer_rsrc_t srdA=mk_srd(wsp+(size_t)tb*128*2048, 128*4096);
    __amdgpu_buffer_rsrc_t srdB=mk_srd(z2t+(size_t)fb*144*2048, 144*4096);
    __amdgpu_buffer_rsrc_t srdX=mk_srd(xn+(size_t)tb*128*D, 128*D*2);
    __amdgpu_buffer_rsrc_t srdW=mk_srd(w1t+((size_t)fb*CGROWS+144)*D, 144*D*2);
    const int sw=((lw&1)<<2)|(lane>>4);
    const unsigned voff=(unsigned)((lane>>3)*4096+(((lane&7)^sw)<<4));
    const unsigned voffz=(unsigned)((lane>>3)*(D*2)+(((lane&7)^sw)<<4));
    const int ordv=korder(lane&31,(ia+ib)&3);
    auto piece=[&](unsigned k,unsigned slot,int j) __attribute__((always_inline)) {
      if(j<4){ const int p=lw+4*j; dma16(srdA,voff,k+p*32768u,slot+p*1024u); }
      else { const int p=lw+4*(j-4); if(j<8||p<18) dma16(srdB,voff,k+p*32768u,slot+16384u+p*1024u); } };
    auto issue=[&](int t) __attribute__((always_inline)) {
      const unsigned slot=ldsbase+(unsigned)(t&3)*SLOT;
      if(t<NZ){ const unsigned k=(unsigned)t*128u;
        #pragma unroll
        for(int j=0;j<4;j++){ const int p=lw+4*j; dma16(srdX,voffz,k+p*(8u*D*2),slot+p*1024u); }
        #pragma unroll
        for(int j=0;j<5;j++){ const int p=lw+4*j; if(j<4||p<18) dma16(srdW,voffz,k+p*(8u*D*2),slot+16384u+p*1024u); }
      } else { const unsigned k=(unsigned)__builtin_amdgcn_readlane(ordv,t-NZ)*128u;
        #pragma unroll
        for(int j=0;j<9;j++) piece(k,slot,j); } };
    issue(0); issue(1); issue(2);
    if(lw<2) WAITV(18); else WAITV(16);
    BARRIER();
    issue(3);
    for(int t=0;t<NT-1;t++){
      if(t<NT-3){ if(lw<2) WAITV(18); else WAITV(16); }
      else if(t==NT-3){ if(lw<2) WAITV(9); else WAITV(8); }
      else WAITV(0);
      BARRIER();
      if(t+4<NT) issue(t+4);
    }
    const int wt=b*4+lw; const bool wact=wt<810; const int wk0=(wact?wt/18:0)*64, wn0=(wact?wt%18:0)*32;
    u32x4 wv[4];
    { const u16* src=(wk0<D?wo+(size_t)wk0*D:wout+(size_t)(wk0-D)*D)+wn0+(lane&3)*8;
      #pragma unroll
      for(int i=0;i<4;i++) asm volatile("global_load_dwordx4 %0, %1, off" : "=v"(wv[i]) : "v"(src+(size_t)((lane>>2)+16*i)*D) : "memory"); }
    BARRIER();
    asm volatile("s_waitcnt vmcnt(0)" : "+v"(wv[0]),"+v"(wv[1]),"+v"(wv[2]),"+v"(wv[3]) :: "memory");
    BARRIER();
    store_tile();
    if(wact){ char* T=shm+TOFF+lw*4352;
      #pragma unroll
      for(int i=0;i<4;i++){ unsigned* p=(unsigned*)(T+((lane>>2)+16*i)*68+(lane&3)*16); p[0]=wv[i][0]; p[1]=wv[i][1]; p[2]=wv[i][2]; p[3]=wv[i][3]; }
      asm volatile("s_waitcnt lgkmcnt(0)" ::: "memory");
      #pragma unroll
      for(int i=0;i<4;i++){ const int n=(lane>>3)+8*i, kc=lane&7; unsigned e[8];
        #pragma unroll
        for(int j=0;j<8;j++) e[j]=*(const u16*)(T+(kc*8+j)*68+n*2);
        u32x4 w; w[0]=e[0]|(e[1]<<16); w[1]=e[2]|(e[3]<<16); w[2]=e[4]|(e[5]<<16); w[3]=e[6]|(e[7]<<16);
        *(u32x4*)(w3t+(size_t)(wn0+n)*LD3+wk0+kc*8)=w; } }
  } else {
    const int fr=lane&15, fq=lane>>4;
    const unsigned o0=(unsigned)(fr*128+(((0+fq)^(fr>>1))<<4));
    const unsigned o1=(unsigned)(fr*128+(((4+fq)^(fr>>1))<<4));
    const unsigned a0=ldsbase+wid*4096+o0, a1=ldsbase+wid*4096+o1, b0=ldsbase+16384+o0, b1=ldsbase+16384+o1;
    f32x4 acc[9][2];
    #pragma unroll
    for(int n=0;n<9;n++){ acc[n][0]=f32x4{0,0,0,0}; acc[n][1]=f32x4{0,0,0,0}; }
    bf16x8 xa[2],xb[9],ya[2],yb[9];
    float bias[2];
    #pragma unroll
    for(int m=0;m<2;m++) bias[m]=b2f(bsp[tb*128+wid*32+m*16+fr]);
    BARRIER();

    auto first=[&](int t) __attribute__((always_inline)) { const unsigned so=(t&3)*SLOT, pa=a0+so, pb=b0+so;
      LDSRD(xa[0],pa,0); LDSRD(xa[1],pa,2048);
      #pragma unroll
      for(int n=0;n<9;n++) LDSRD(xb[n],pb,n*2048);
      WAITL0(); };
    first(0);
    for(int t=0;t<NZ-1;t++){
      const unsigned so=(t&3)*SLOT, sn=((t+1)&3)*SLOT;
      mphase<9,false,true>(acc,xa,xb,ya,yb,a1+so,b1+so);
      WAITL0_BARRIER();
      mphase<9,false,true>(acc,ya,yb,xa,xb,a0+sn,b0+sn);
      WAITL0();
    }
    { const unsigned so=((NZ-1)&3)*SLOT;
      mphase<9,false,true>(acc,xa,xb,ya,yb,a1+so,b1+so);
      WAITL0_BARRIER();
      mphase<9,false,false>(acc,ya,yb,xa,xb,0,0); }
    MFMA_DRAIN();
    unsigned stash[9][2][2];
    #pragma unroll
    for(int n=0;n<9;n++)
      #pragma unroll
      for(int m=0;m<2;m++){ const f32x2 y0=gelu2(f32x2{acc[n][m][0],acc[n][m][1]}), y1=gelu2(f32x2{acc[n][m][2],acc[n][m][3]});
        stash[n][m][0]=pack2(y0[0],y0[1]); stash[n][m][1]=pack2(y1[0],y1[1]); PIN(stash[n][m][0]); PIN(stash[n][m][1]);
        acc[n][m]=f32x4{0,0,0,0}; }

    first(NZ);
    for(int t=NZ;t<NT-1;t++){
      const unsigned so=(t&3)*SLOT, sn=((t+1)&3)*SLOT;
      mphase<9,false,true>(acc,xa,xb,ya,yb,a1+so,b1+so);
      WAITL0_BARRIER();
      mphase<9,false,true>(acc,ya,yb,xa,xb,a0+sn,b0+sn);
      WAITL0();
    }
    { const unsigned so=((NT-1)&3)*SLOT;
      mphase<9,false,true>(acc,xa,xb,ya,yb,a1+so,b1+so);
      WAITL0_BARRIER();
      mphase<9,false,false>(acc,ya,yb,xa,xb,0,0); }
    MFMA_DRAIN();

    #pragma unroll
    for(int m=0;m<2;m++){
      const int tl=wid*32+m*16+fr;
      #pragma unroll
      for(int n=0;n<9;n++){
        char* p=shm+tl*288+(n*16+fq*4)*2;
        u32x2 zz; zz[0]=stash[n][m][0]; zz[1]=stash[n][m][1]; u32x2 hh;
        const f32x2 bb={bias[m],bias[m]};
        const f32x2 h0=f32x2{lo2f(zz[0]),hi2f(zz[0])}*(f32x2{acc[n][m][0],acc[n][m][1]}+bb), h1=f32x2{lo2f(zz[1]),hi2f(zz[1])}*(f32x2{acc[n][m][2],acc[n][m][3]}+bb);
        hh[0]=pack2(h0[0],h0[1]); hh[1]=pack2(h1[0],h1[1]);
        *(u32x2*)p=hh;
      }
    }

    WAITL0_BARRIER();

    store_tile();
  }

}
}
namespace k3 {
constexpr int SLOT=36864;
constexpr int NSLOT=4, LDS_BYTES=NSLOT*SLOT, NT=23;
__global__ __launch_bounds__(512,2) void kernel(const u16* __restrict__ a3,const u16* __restrict__ w3t,const u16* __restrict__ x,u16* __restrict__ out){
  extern __shared__ __attribute__((aligned(16))) char shm[];
  const int tid=threadIdx.x, wid=__builtin_amdgcn_readfirstlane(tid>>6), lane=tid&63;
  const int b=blockIdx.x, xcd=b&7, i0=b>>3;
  const int rb_=(xcd&3)*8+(i0&7), cb=(xcd>>2)*4+(i0>>3);
  const unsigned ldsbase=(unsigned)(size_t)shm;

  if(wid>=4){
    const int lw=wid-4;
    __amdgpu_buffer_rsrc_t srdA=mk_srd(a3+(size_t)rb_*64*LD3, 64*LD3*2);
    __amdgpu_buffer_rsrc_t srdB=mk_srd(w3t+(size_t)cb*72*LD3, 72*LD3*2);
    const unsigned voff=(unsigned)((lane>>4)*(LD3*2)+(((lane&15)^(4*lw+(lane>>4)))<<4));
    auto issue=[&](int t){
      const unsigned k=(unsigned)t*256u; const unsigned slot=ldsbase+(unsigned)(t&3)*SLOT;
      #pragma unroll
      for(int j=0;j<4;j++){ const int p=lw+4*j; dma16(srdA,voff,k+p*(4u*LD3*2),slot+p*1024u); }
      #pragma unroll
      for(int j=0;j<5;j++){ const int p=lw+4*j; if(j<4||p<18) dma16(srdB,voff,k+p*(4u*LD3*2),slot+16384u+p*1024u); }
    };
    issue(0); issue(1); issue(2);
    if(lw<2) WAITV(18); else WAITV(16);
    BARRIER();
    issue(3);
    for(int t=0;t<NT-1;t++){
      if(t<NT-3){ if(lw<2) WAITV(18); else WAITV(16); }
      else if(t==NT-3){ if(lw<2) WAITV(9); else WAITV(8); }
      else WAITV(0);
      BARRIER();
      if(t+4<NT) issue(t+4);
    }
    BARRIER();
    BARRIER();
  } else {
    const int fr=lane&15, fq=lane>>4;
    const unsigned o=(unsigned)(fr*256+(((wid*4+fq)^fr)<<4));
    const unsigned a0=ldsbase+o, b0=ldsbase+16384+o;
    f32x4 acc[5][4];
    #pragma unroll
    for(int n=0;n<5;n++)
      #pragma unroll
      for(int m=0;m<4;m++) acc[n][m]=f32x4{0,0,0,0};
    bf16x8 xa[4],xb[5],ya[4],yb[5];
    #define K3PH(CA,CB,NA,NB_,PA,PB,RD_) { \
      MFMA(acc[0][0],CB[0],CA[0]); MFMA(acc[0][1],CB[0],CA[1]); if(RD_){ LDSRD(NA[0],PA,0); LDSRD(NA[1],PA,4096); } \
      MFMA(acc[0][2],CB[0],CA[2]); MFMA(acc[0][3],CB[0],CA[3]); if(RD_){ LDSRD(NA[2],PA,8192); LDSRD(NA[3],PA,12288); } \
      MFMA(acc[1][0],CB[1],CA[0]); MFMA(acc[1][1],CB[1],CA[1]); if(RD_){ LDSRD(NB_[0],PB,0); LDSRD(NB_[1],PB,4096); } \
      MFMA(acc[1][2],CB[1],CA[2]); MFMA(acc[1][3],CB[1],CA[3]); if(RD_){ LDSRD(NB_[2],PB,8192); LDSRD(NB_[3],PB,12288); } \
      MFMA(acc[2][0],CB[2],CA[0]); MFMA(acc[2][1],CB[2],CA[1]); if(RD_){ LDSRD(NB_[4],PB,16384); } \
      MFMA(acc[2][2],CB[2],CA[2]); MFMA(acc[2][3],CB[2],CA[3]); \
      MFMA(acc[3][0],CB[3],CA[0]); MFMA(acc[3][1],CB[3],CA[1]); MFMA(acc[3][2],CB[3],CA[2]); MFMA(acc[3][3],CB[3],CA[3]); \
      MFMA(acc[4][0],CB[4],CA[0]); MFMA(acc[4][1],CB[4],CA[1]); MFMA(acc[4][2],CB[4],CA[2]); MFMA(acc[4][3],CB[4],CA[3]); }
    BARRIER();

    LDSRD(xa[0],a0,0); LDSRD(xa[1],a0,4096); LDSRD(xa[2],a0,8192); LDSRD(xa[3],a0,12288);
    LDSRD(xb[0],b0,0); LDSRD(xb[1],b0,4096); LDSRD(xb[2],b0,8192); LDSRD(xb[3],b0,12288); LDSRD(xb[4],b0,16384);
    for(int t=0;t<NT-1;t+=2){
      WAITL0_BARRIER();
      { const unsigned sn=((t+1)&3)*SLOT, pa=a0+sn, pb=b0+sn; K3PH(xa,xb,ya,yb,pa,pb,true); }
      WAITL0_BARRIER();

      { const unsigned sn=((t+2)&3)*SLOT, pa=a0+sn, pb=b0+sn; K3PH(ya,yb,xa,xb,pa,pb,true); }
    }
    WAITL0_BARRIER();
    K3PH(xa,xb,ya,yb,a0,b0,false);
    MFMA_DRAIN();

    float* P=(float*)(shm)+wid*(64*84);
    #pragma unroll
    for(int n=0;n<5;n++)
      #pragma unroll
      for(int m=0;m<4;m++) *(f32x4*)(P+(m*16+fr)*84+n*16+fq*4)=acc[n][m];
    WAITL0_BARRIER();
  }

  for(int id=tid;id<576;id+=512){
    const int r=(id*7282)>>16, c=id-r*9;
    const float* P=(const float*)(shm)+r*84+c*8;
    f32x4 s0={0,0,0,0},s1={0,0,0,0};
    #pragma unroll
    for(int w=0;w<4;w++){ s0+=*(const f32x4*)(P+w*64*84); s1+=*(const f32x4*)(P+w*64*84+4); }
    const size_t go=(size_t)(rb_*64+r)*D+cb*72+c*8;
    const u32x4 xv=*(const u32x4*)(x+go); u32x4 o;
    o[0]=pack2(lo2f(xv[0])+s0[0],hi2f(xv[0])+s0[1]); o[1]=pack2(lo2f(xv[1])+s0[2],hi2f(xv[1])+s0[3]);
    o[2]=pack2(lo2f(xv[2])+s1[0],hi2f(xv[2])+s1[1]); o[3]=pack2(lo2f(xv[3])+s1[2],hi2f(xv[3])+s1[3]);
    *(u32x4*)(out+go)=o;
  }

}
}
extern "C" void kernel_launch(void* const* d_in, const int* in_sizes, int n_in, void* d_out, int out_size, void* d_ws, size_t ws_size, hipStream_t stream) {
  const u16* x=(const u16*)d_in[0]; const u16* wqkv=(const u16*)d_in[1]; const u16* wo=(const u16*)d_in[2];
  const u16* w_in=(const u16*)d_in[4]; const u16* w_sp=(const u16*)d_in[5]; const u16* b_sp=(const u16*)d_in[6]; const u16* w_out=(const u16*)d_in[7];
  char* ws=(char*)d_ws;
  unsigned long long* part=(unsigned long long*)ws;
  u16* xn =(u16*)(ws+262144);
  u16* w1t=xn+(size_t)S*D;
  u16* w3t=w1t+(size_t)16*CGROWS*D;
  u16* a3 =w3t+(size_t)D*LD3;
  u16* z2t=a3+(size_t)S*LD3;
  (void)hipFuncSetAttribute((const void*)k1::kernel,hipFuncAttributeMaxDynamicSharedMemorySize,k1::LDS_BYTES);
  (void)hipFuncSetAttribute((const void*)k2::kernel,hipFuncAttributeMaxDynamicSharedMemorySize,k2::LDS_BYTES);
  (void)hipFuncSetAttribute((const void*)k3::kernel,hipFuncAttributeMaxDynamicSharedMemorySize,k3::LDS_BYTES);
  k0::kernel<<<k0::GRID,256,0,stream>>>(x,wqkv,w_in,xn,w1t,w3t,a3,(u32x4*)part);
  k1::kernel<<<256,512,k1::LDS_BYTES,stream>>>(xn,w1t,z2t,a3,part,w_sp);
  k2::kernel<<<256,512,k2::LDS_BYTES,stream>>>(w_sp,z2t,xn,w1t,b_sp,a3+D,wo,w_out,w3t);
  k3::kernel<<<256,512,k3::LDS_BYTES,stream>>>(a3,w3t,x,(u16*)d_out);
}
```
